```python
import torch

def _softmax(x, axis=-1):
    return torch.softmax(x, dim=axis)
def _gelu(x):
    return torch.nn.functional.gelu(x, approximate="tanh")
def _silu(x):
    return torch.nn.functional.silu(x)
def _sigmoid(x):
    return torch.sigmoid(x)
def _layernorm(x, axis=-1, eps=1e-5):
    m = x.mean(dim=axis, keepdim=True)
    v = x.var(dim=axis, keepdim=True, unbiased=False)
    return (x - m) / torch.sqrt(v + eps)
def _scatter_add(tgt, idx, src, axis=0):
    return tgt.clone().index_add_(axis, idx, src)
def _segment_sum(src, seg, n):
    out = torch.zeros((n,) + src.shape[1:], dtype=src.dtype)
    return out.index_add_(0, seg, src)
def _pad(x, pw):
    flat = tuple(v for pair in reversed(pw) for v in pair)
    return torch.nn.functional.pad(x, flat)

kernel_name = 'parallel_sum__mha_block_cached__geglu_ffn__decode'

def setup_inputs():

    _w = (lambda a: (lambda t: t.to(torch.bfloat16) if t.is_floating_point() else t)(torch.as_tensor(a)))
    def _A_setup():
        B, Sq, D = 2, 1, 512
        H, Sk, Dh = 32, 4096, 128
        return {
            "x": _w((lambda *s: torch.randn(*s, dtype=torch.bfloat16) * 0.05)(B, Sq, D)),
            "k_cache": _w((lambda *s: torch.randn(*s, dtype=torch.bfloat16) * 3.2)(B, H, Sk, Dh)),
            "v_cache": _w((lambda *s: torch.randn(*s, dtype=torch.bfloat16) * 1.6)(B, H, Sk, Dh)),
            "wq": _w((lambda *s: torch.randn(*s, dtype=torch.bfloat16) * 3.2)(D, H * Dh) * (D ** -0.5)),
            "wo": _w((lambda *s: torch.randn(*s, dtype=torch.bfloat16) * 1.6)(H * Dh, D) * ((H * Dh) ** -0.5)),
        }
    _A_in = _A_setup()

    def _B_setup():
        B, S, D, F = 2, 1, 512, 1376
        return {
            "x": _w((lambda *s: torch.randn(*s, dtype=torch.bfloat16) * 0.05)(B, S, D)),
            "w_gate": _w((lambda *s: torch.randn(*s, dtype=torch.bfloat16) * 0.05)(D, F)),
            "w_up": _w((lambda *s: torch.randn(*s, dtype=torch.bfloat16) * 0.05)(D, F)),
            "w_down": _w((lambda *s: torch.randn(*s, dtype=torch.bfloat16) * 0.05)(F, D)),
        }
    _B_in = _B_setup()

    return {
        "A_x": _A_in["x"],
        "A_k_cache": _A_in["k_cache"],
        "A_v_cache": _A_in["v_cache"],
        "A_wq": _A_in["wq"],
        "A_wo": _A_in["wo"],
        "B_x": _B_in["x"],
        "B_w_gate": _B_in["w_gate"],
        "B_w_up": _B_in["w_up"],
        "B_w_down": _B_in["w_down"],
    }

def reference(A_x, A_k_cache, A_v_cache, A_wq, A_wo, B_x, B_w_gate, B_w_up, B_w_down):
    def _A_ref(x, k_cache, v_cache, wq, wo):
        B, Sq, D = x.shape
        H, Dh = 32, 128
        q = (lambda x, *d: x.permute(*d))((x @ wq).reshape(B, Sq, H, Dh), 0, 2, 1, 3)
        scores = torch.einsum("bhqd,bhkd->bhqk", q, k_cache) * (Dh ** -0.5)
        p = _softmax(scores, axis=-1)
        o = torch.einsum("bhqk,bhkd->bhqd", p, v_cache)
        return (lambda x, *d: x.permute(*d))(o, 0, 2, 1, 3).reshape(B, Sq, H * Dh) @ wo

    def _B_ref(x, w_gate, w_up, w_down):
        return (_gelu(x @ w_gate) * (x @ w_up)) @ w_down

    x = A_x
    xn = _layernorm(x, axis=-1)
    return x + _A_ref(xn, A_k_cache, A_v_cache, A_wq, A_wo) + _B_ref(xn, B_w_gate, B_w_up, B_w_down)
```

```cpp
#include <hip/hip_runtime.h>
#include <hip/hip_bf16.h>
#include <stdint.h>

#define MIMIC 0

typedef unsigned u32x4 __attribute__((ext_vector_type(4)));
typedef float f32x4 __attribute__((ext_vector_type(4)));
using bf16x2 = __attribute__((ext_vector_type(2))) __bf16;

constexpr int B = 2, D = 512, H = 32, SK = 4096, DH = 128, F = 1376;
constexpr int NSPLIT = 4;

__device__ __forceinline__ float bf2f(unsigned short v) { return __uint_as_float(((unsigned)v) << 16); }
__device__ __forceinline__ float rbf(float f) {
  unsigned u = __float_as_uint(f);
  u += 0x7FFFu + ((u >> 16) & 1u);
  return __uint_as_float(u & 0xffff0000u);
}
__device__ __forceinline__ unsigned short f2bf(float f) {
  unsigned u = __float_as_uint(f);
  u += 0x7FFFu + ((u >> 16) & 1u);
  return (unsigned short)(u >> 16);
}
__device__ __forceinline__ float dot2(unsigned k, unsigned q, float acc) {
  return __builtin_amdgcn_fdot2_f32_bf16(__builtin_bit_cast(bf16x2, k), __builtin_bit_cast(bf16x2, q), acc, false);
}

__device__ void layernorm_to_lds(const unsigned short* x, float* xn  , int tid, int nthr) {
  int lane = tid & 63;
  for (int b = 0; b < B; ++b) {
    float v[8]; float s = 0.f;
    for (int j = 0; j < 8; ++j) { v[j] = bf2f(x[b * D + lane * 8 + j]); s += v[j]; }
    for (int m = 32; m >= 1; m >>= 1) s += __shfl_xor(s, m);
    float mean = s * (1.f / D);
    float q = 0.f;
    for (int j = 0; j < 8; ++j) { float d = v[j] - mean; q += d * d; }
    for (int m = 32; m >= 1; m >>= 1) q += __shfl_xor(q, m);
    float var = q * (1.f / D);
    float mb = rbf(mean), vb = rbf(var);
    float den = rbf(sqrtf(rbf(vb + 1e-5f)));
    if (tid < 64) for (int j = 0; j < 8; ++j) xn[b * D + lane * 8 + j] = rbf(rbf(v[j] - mb) / den);
  }
}

__global__ void __launch_bounds__(256) k_pre(const unsigned short* x, const unsigned short* wq, const unsigned short* wg, const unsigned short* wu,
                                             float* q, float* hm) {
  __shared__ float xn[2 * D];
  __shared__ float red[4][2][2][64];
  int tid = threadIdx.x;
  layernorm_to_lds(x, xn, tid, 256);
  __syncthreads();
  int w = tid >> 6, lane = tid & 63;
  if (blockIdx.x < 64) {
    int col = blockIdx.x * 64 + lane;
    float a0 = 0, a1 = 0;
    for (int k = w * 128; k < w * 128 + 128; ++k) { float wv = bf2f(wq[(size_t)k * 4096 + col]); a0 += xn[k] * wv; a1 += xn[D + k] * wv; }
    red[w][0][0][lane] = a0; red[w][0][1][lane] = a1;
    __syncthreads();
    if (tid < 128) { int b = tid >> 6; float s = red[0][0][b][lane] + red[1][0][b][lane] + red[2][0][b][lane] + red[3][0][b][lane];
      q[b * 4096 + col] = rbf(s); }
  } else {
    int col = (blockIdx.x - 64) * 64 + lane;
    float g0 = 0, g1 = 0, u0 = 0, u1 = 0;
    if (col < F) for (int k = w * 128; k < w * 128 + 128; ++k) {
      float gv = bf2f(wg[(size_t)k * F + col]), uv = bf2f(wu[(size_t)k * F + col]);
      g0 += xn[k] * gv; g1 += xn[D + k] * gv; u0 += xn[k] * uv; u1 += xn[D + k] * uv; }
    red[w][0][0][lane] = g0; red[w][0][1][lane] = g1; red[w][1][0][lane] = u0; red[w][1][1][lane] = u1;
    __syncthreads();
    if (tid < 128 && col < F) { int b = tid >> 6;
      float g = rbf(red[0][0][b][lane] + red[1][0][b][lane] + red[2][0][b][lane] + red[3][0][b][lane]);
      float u = rbf(red[0][1][b][lane] + red[1][1][b][lane] + red[2][1][b][lane] + red[3][1][b][lane]);
      float t = tanhf(0.7978845608028654f * (g + 0.044715f * g * g * g));
      float ge = rbf(0.5f * g * (1.f + t));
      hm[b * F + col] = rbf(ge * u); }
  }
}

__global__ void __launch_bounds__(512, 2) k_attn(const unsigned short* kc, const unsigned short* vc, const float* q, float* part  ) {
  __shared__ float lm[8], ll[8], lo[8][128];
  int tid = threadIdx.x, lane = tid & 63, w = tid >> 6;
  int bh = blockIdx.x >> 2, sp = blockIdx.x & 3;
  int r = lane >> 4, c = lane & 15;
  size_t base = ((size_t)bh * SK + sp * 1024 + w * 128) * DH;
  const u32x4* kp = (const u32x4*)(kc + base) + lane;
  const u32x4* vp = (const u32x4*)(vc + base) + lane;
  u32x4 K[32], V[16];
#pragma unroll
  for (int i = 0; i < 32; ++i) K[i] = __builtin_nontemporal_load(kp + i * 64);
#pragma unroll
  for (int i = 0; i < 16; ++i) V[i] = __builtin_nontemporal_load(vp + i * 64);
  __builtin_amdgcn_sched_barrier(0);
  unsigned qp[4];
  { int b = bh >> 5, h = bh & 31; const float* qq = q + b * 4096 + h * 128 + c * 8;
    for (int j = 0; j < 4; ++j) qp[j] = (__float_as_uint(qq[2 * j]) >> 16) | (__float_as_uint(qq[2 * j + 1]) & 0xffff0000u); }
  float s[32];
#pragma unroll
  for (int i = 0; i < 32; ++i) { float d = 0; for (int j = 0; j < 4; ++j) d = dot2(K[i][j], qp[j], d);
    d += __shfl_xor(d, 1); d += __shfl_xor(d, 2); d += __shfl_xor(d, 4); d += __shfl_xor(d, 8);
    s[i] = d * 0.08838834764831845f;
  }
  u32x4 V2[16];
#pragma unroll
  for (int i = 0; i < 16; ++i) V2[i] = __builtin_nontemporal_load(vp + (16 + i) * 64);
  float m = s[0];
#pragma unroll
  for (int i = 1; i < 32; ++i) m = fmaxf(m, s[i]);
  m = fmaxf(m, __shfl_xor(m, 16)); m = fmaxf(m, __shfl_xor(m, 32));
  float l = 0; float acc[8] = {0, 0, 0, 0, 0, 0, 0, 0};
#pragma unroll
  for (int i = 0; i < 32; ++i) { float p = __expf(s[i] - m); l += p; u32x4 v = i < 16 ? V[i] : V2[i - 16];
    for (int j = 0; j < 4; ++j) { acc[2 * j] += p * __uint_as_float(v[j] << 16); acc[2 * j + 1] += p * __uint_as_float(v[j] & 0xffff0000u); } }
  l += __shfl_xor(l, 16); l += __shfl_xor(l, 32);
  for (int j = 0; j < 8; ++j) { acc[j] += __shfl_xor(acc[j], 16); acc[j] += __shfl_xor(acc[j], 32); }
  if (r == 0) { for (int j = 0; j < 8; ++j) lo[w][c * 8 + j] = acc[j]; if (c == 0) { lm[w] = m; ll[w] = l; } }
  __syncthreads();
  if (tid < 128) { float M = lm[0]; for (int i = 1; i < 8; ++i) M = fmaxf(M, lm[i]);
    float L = 0, o = 0; for (int i = 0; i < 8; ++i) { float e = __expf(lm[i] - M); L += e * ll[i]; o += e * lo[i][tid]; }
    part[blockIdx.x * 132 + tid] = o; if (tid == 0) { part[blockIdx.x * 132 + 128] = M; part[blockIdx.x * 132 + 129] = L; } }
}

__global__ void __launch_bounds__(256) k_post(const unsigned short* x, const unsigned short* wo, const unsigned short* wd, const float* part, const float* hm, unsigned short* out) {
  __shared__ float o[2][4096];
  __shared__ float red[256][2][2][8];
  int tid = threadIdx.x;
  for (int i = tid; i < 2 * 4096; i += 256) { int bh = i >> 7, d = i & 127; const float* p = part + bh * 4 * 132;
    float M = fmaxf(fmaxf(p[128], p[132 + 128]), fmaxf(p[264 + 128], p[396 + 128])); float L = 0, a = 0;
    for (int s = 0; s < 4; ++s) { float e = __expf(p[s * 132 + 128] - M); L += e * p[s * 132 + 129]; a += e * p[s * 132 + d]; }
    o[0][i] = rbf(a / L); }
  __syncthreads();
  int c0 = blockIdx.x * 8;
  float a[2][8] = {}, f[2][8] = {};
  for (int k = tid; k < 4096; k += 256) { u32x4 wv = *(const u32x4*)(wo + (size_t)k * D + c0);
    for (int j = 0; j < 4; ++j) { float lo_ = __uint_as_float(wv[j] << 16), hi_ = __uint_as_float(wv[j] & 0xffff0000u);
      a[0][2 * j] += o[0][k] * lo_; a[0][2 * j + 1] += o[0][k] * hi_; a[1][2 * j] += o[1][k] * lo_; a[1][2 * j + 1] += o[1][k] * hi_; } }
  for (int k = tid; k < F; k += 256) { u32x4 wv = *(const u32x4*)(wd + (size_t)k * D + c0); float h0 = hm[k], h1 = hm[F + k];
    for (int j = 0; j < 4; ++j) { float lo_ = __uint_as_float(wv[j] << 16), hi_ = __uint_as_float(wv[j] & 0xffff0000u);
      f[0][2 * j] += h0 * lo_; f[0][2 * j + 1] += h0 * hi_; f[1][2 * j] += h1 * lo_; f[1][2 * j + 1] += h1 * hi_; } }
  for (int b = 0; b < 2; ++b) for (int j = 0; j < 8; ++j) { red[tid][0][b][j] = a[b][j]; red[tid][1][b][j] = f[b][j]; }
  __syncthreads();
  if (tid < 16) { int b = tid >> 3, j = tid & 7; float sa = 0, sf = 0; for (int t = 0; t < 256; ++t) { sa += red[t][0][b][j]; sf += red[t][1][b][j]; }
    float xv = bf2f(x[b * D + c0 + j]);
    out[b * D + c0 + j] = f2bf(rbf(xv + rbf(sa)) + rbf(sf)); }
}

extern "C" void kernel_launch(void* const* d_in, const int* in_sizes, int n_in, void* d_out, int out_size, void* d_ws, size_t ws_size, hipStream_t stream) {
  const unsigned short* x = (const unsigned short*)d_in[0];
  const unsigned short* kc = (const unsigned short*)d_in[1];
  const unsigned short* vc = (const unsigned short*)d_in[2];
  const unsigned short* wq = (const unsigned short*)d_in[3];
  const unsigned short* wo = (const unsigned short*)d_in[4];
  const unsigned short* wg = (const unsigned short*)d_in[6];
  const unsigned short* wu = (const unsigned short*)d_in[7];
  const unsigned short* wd = (const unsigned short*)d_in[8];
  float* q = (float*)d_ws; float* hm = q + 8192; float* part = hm + 4096;
  k_pre<<<64 + 22, 256, 0, stream>>>(x, wq, wg, wu, q, hm);
  k_attn<<<256, 512, 0, stream>>>(kc, vc, q, part);
  k_post<<<64, 256, 0, stream>>>(x, wo, wd, part, hm, (unsigned short*)d_out);
}
```

```cpp
#include <hip/hip_runtime.h>
#include <stdint.h>

constexpr int PREQ = 48;
constexpr int WPREQ = 23;
constexpr int BARI = 5;
constexpr int KB = 3, VB = 2;
constexpr int KL = 2;
constexpr int WMIN = 4, WMID = 6, WMAX = 10, LSH = 5, LSHE = 4, NLE = 100, SCHD = 50, SCHQ = 55;
constexpr int NCK = 32, NCK1 = 64, NCK2 = 84, DK1 = -10, DK2 = -6, TGAT = 240, TGAT2 = 100;
constexpr float BETA0 = 0.f, BETA1 = 0.25f, BETA2 = 0.5f;

typedef unsigned u32x4 __attribute__((ext_vector_type(4)));
typedef unsigned u32x2 __attribute__((ext_vector_type(2)));
typedef float f32x4 __attribute__((ext_vector_type(4)));
typedef float f32x2 __attribute__((ext_vector_type(2)));
typedef __bf16 bf16x8 __attribute__((ext_vector_type(8)));
typedef __bf16 bf16x2 __attribute__((ext_vector_type(2)));
typedef __amdgpu_buffer_rsrc_t rsrc_t;
#define LAS __attribute__((address_space(3)))

constexpr int NSW = 4, NL = 64, NG = 4;
constexpr int AUX_NT = 2, AUX_SC1 = 16;
constexpr unsigned SPIN_CAP = 1u << 19;
constexpr int WS_EPOCH = 0;
constexpr int WS_T = 1024;
constexpr int WS_ML = 4096;
constexpr int WS_O = WS_ML + 256 * 128;
constexpr int WS_Q = WS_O + 256 * 512;
constexpr int WS_G = WS_Q + 256 * 1024;
constexpr int WS_Y = WS_G + 22 * 8 * 1024;
constexpr int WS_END = WS_Y + 256 * 1024;
constexpr int OOB = 0x40000000;
constexpr int L_Q = 0, L_ML = 256, L_OF = 288, L_IF = 304, L_QF = 320, L_T = 324, L_TAG = 336, L_H = 512, L_O = 768, L_XN = L_O + 2048, L_END = L_XN + 2048;

__device__ __forceinline__ rsrc_t mk(const void* p, int bytes) { return __builtin_amdgcn_make_buffer_rsrc((void*)p, 0, bytes, 0x00020000); }
template <int AUX> __device__ __forceinline__ u32x4 ld16(rsrc_t r, int voff, int soff) { return __builtin_amdgcn_raw_buffer_load_b128(r, voff, soff, AUX); }
__device__ __forceinline__ float lo16(unsigned v) { return __uint_as_float(v << 16); }
__device__ __forceinline__ float hi16(unsigned v) { return __uint_as_float(v & 0xffff0000u); }
__device__ __forceinline__ unsigned pk(float lo, float hi) { bf16x2 r = {(__bf16)lo, (__bf16)hi}; return __builtin_bit_cast(unsigned, r); }
__device__ __forceinline__ float rbf(float f) { return lo16(pk(f, f)); }
__device__ __forceinline__ float dot2(unsigned a, unsigned b, float c) {
  return __builtin_amdgcn_fdot2_f32_bf16(__builtin_bit_cast(bf16x2, a), __builtin_bit_cast(bf16x2, b), c, false);
}
template <int CTRL> __device__ __forceinline__ float dpp(float x) {
  return __builtin_bit_cast(float, __builtin_amdgcn_mov_dpp(__builtin_bit_cast(int, x), CTRL, 0xf, 0xf, true));
}
constexpr int XOR1 = 0xB1, XOR2 = 0x4E, XOR7 = 0x141, ROR8 = 0x128, ROR4 = 0x124, BCAST = 0x150;
__device__ __forceinline__ float sw32_add(float a, float b) {
  auto t = __builtin_amdgcn_permlane32_swap(__float_as_uint(a), __float_as_uint(b), false, false);
  return __uint_as_float(t[0]) + __uint_as_float(t[1]);
}
__device__ __forceinline__ float sw16_add(float a, float b) {
  auto t = __builtin_amdgcn_permlane16_swap(__float_as_uint(a), __float_as_uint(b), false, false);
  return __uint_as_float(t[0]) + __uint_as_float(t[1]);
}
__device__ __forceinline__ float wave_max(float m) {
  m = fmaxf(m, dpp<ROR8>(m)); m = fmaxf(m, dpp<ROR4>(m)); m = fmaxf(m, dpp<XOR2>(m)); m = fmaxf(m, dpp<XOR1>(m));
  auto s = __builtin_amdgcn_permlane16_swap(__float_as_uint(m), __float_as_uint(m), false, false);
  m = fmaxf(__uint_as_float(s[0]), __uint_as_float(s[1]));
  auto t = __builtin_amdgcn_permlane32_swap(__float_as_uint(m), __float_as_uint(m), false, false);
  return fmaxf(__uint_as_float(t[0]), __uint_as_float(t[1]));
}
__device__ __forceinline__ float wave_sum(float v) {
  v += dpp<ROR8>(v); v += dpp<ROR4>(v); v += dpp<XOR2>(v); v += dpp<XOR1>(v);
  v = sw16_add(v, v); return sw32_add(v, v);
}
__device__ __forceinline__ f32x4 mfma(u32x4 a, u32x4 b, f32x4 c) {
  return __builtin_amdgcn_mfma_f32_16x16x32_bf16(__builtin_bit_cast(bf16x8, a), __builtin_bit_cast(bf16x8, b), c, 0, 0, 0);
}
template <int E> __device__ __forceinline__ u32x4 colfrag(const u32x4* w) {
  constexpr unsigned sel = (E & 1) ? 0x07060302u : 0x05040100u;
  return u32x4{__builtin_amdgcn_perm(w[1][E >> 1], w[0][E >> 1], sel), __builtin_amdgcn_perm(w[3][E >> 1], w[2][E >> 1], sel),
               __builtin_amdgcn_perm(w[5][E >> 1], w[4][E >> 1], sel), __builtin_amdgcn_perm(w[7][E >> 1], w[6][E >> 1], sel)};
}
template <int E> __device__ __forceinline__ float gv2(u32x4 a0, u32x4 a1, const u32x4* w) {
  return mfma(a1, colfrag<E>(w + 8), mfma(a0, colfrag<E>(w), f32x4{0.f, 0.f, 0.f, 0.f}))[0];
}
__device__ __forceinline__ f32x4 tile2(u32x4 a0, u32x4 a1, const u32x4* w, bool eh) {
  const float z0 = gv2<0>(a0, a1, w), z4 = gv2<4>(a0, a1, w), z1 = gv2<1>(a0, a1, w), z5 = gv2<5>(a0, a1, w);
  const float z2 = gv2<2>(a0, a1, w), z6 = gv2<6>(a0, a1, w), z3 = gv2<3>(a0, a1, w), z7 = gv2<7>(a0, a1, w);
  return f32x4{eh ? z4 : z0, eh ? z5 : z1, eh ? z6 : z2, eh ? z7 : z3};
}
template <int ROWB> __device__ __forceinline__ void tile_load(u32x4* w, rsrc_t r, int voff) {
#pragma unroll
  for (int i = 0; i < 16; ++i) w[i] = ld16<AUX_NT>(r, voff, ((i >> 3) * 32 + (i & 7)) * ROWB);
}
__device__ __forceinline__ void st_pair(rsrc_t r, int off, unsigned a, unsigned b, unsigned tag) {
  __builtin_amdgcn_raw_buffer_store_b128(u32x4{a, tag, b, tag}, r, off, 0, AUX_SC1);
}
__device__ __forceinline__ bool tag_ok(u32x4 v, unsigned tag) { return (v[1] == tag) & (v[3] == tag); }
template <unsigned TM> __device__ __forceinline__ void st_quad(rsrc_t r, int off, f32x4 z, u32x4 t4) {
  u32x4 v;
#pragma unroll
  for (int i = 0; i < 4; ++i) v[i] = (__float_as_uint(z[i]) & ~TM) | t4[i];
  __builtin_amdgcn_raw_buffer_store_b128(v, r, off, 0, AUX_SC1);
}
#define SB() __builtin_amdgcn_sched_barrier(0)
template <int N> __device__ __forceinline__ void vmwait() { asm volatile("s_waitcnt vmcnt(%0)" ::"n"(N) : "memory"); }
__device__ __forceinline__ void lgkm0() { asm volatile("s_waitcnt lgkmcnt(0)" ::: "memory"); }
__device__ __forceinline__ unsigned now() { return (unsigned)__builtin_amdgcn_s_memrealtime(); }

template <int N> struct IC { static constexpr int value = N; };
template <int I, int N, class F> __device__ __forceinline__ void static_for_(F& f) { if constexpr (I < N) { f(IC<I>{}); static_for_<I + 1, N>(f); } }
template <int N, class F> __device__ __forceinline__ void static_for(F f) { static_for_<0, N>(f); }
struct Sched { int nstep, smstep, kbeg[160], kend[160], vbeg[160], vend[160], fin[160]; };
constexpr Sched make_sched() {
  Sched s{}; int kd = 0, vd = 0, t = 0; s.smstep = -1;
  for (; vd < NL; ++t) {
    const int landed = PREQ + t - WMAX;
    s.kbeg[t] = kd;
    s.fin[t] = (t > 0 && s.kend[t - 1] > s.kbeg[t - 1] && (kd & 15) == 0) ? kd / 16 - 1 : -1;
    for (int b = 0; b < (s.fin[t] >= 0 ? 1 : KB) && kd < NL && kd <= landed; ++b) { ++kd; if ((kd & 15) == 0) break; }
    s.kend[t] = kd;
    s.vbeg[t] = vd;
    if (s.smstep >= 0 && t > s.smstep) for (int b = 0; b < VB && vd < NL && NL + vd <= landed; ++b) ++vd;
    s.vend[t] = vd;
    if (s.fin[t] == NG - 1) s.smstep = t + 1;
  }
  s.nstep = t;
  return s;
}
constexpr Sched SCH = make_sched();

__device__ __forceinline__ void layernorm(u32x4 xa, u32x4 xb, LAS unsigned char* dst) {
  float v[16];
#pragma unroll
  for (int i = 0; i < 4; ++i) { v[2 * i] = lo16(xa[i]); v[2 * i + 1] = hi16(xa[i]); v[8 + 2 * i] = lo16(xb[i]); v[9 + 2 * i] = hi16(xb[i]); }
  float s = 0.f;
#pragma unroll
  for (int i = 0; i < 16; ++i) s += v[i];
  s += dpp<XOR1>(s); s += dpp<XOR2>(s); s += dpp<ROR8>(s); s = sw16_add(s, s); s = sw32_add(s, s);
  const float mean = s * (1.f / 512.f);
  float q = 0.f;
#pragma unroll
  for (int i = 0; i < 16; ++i) { const float d = v[i] - mean; q = fmaf(d, d, q); }
  q += dpp<XOR1>(q); q += dpp<XOR2>(q); q += dpp<ROR8>(q); q = sw16_add(q, q); q = sw32_add(q, q);
  const float mb = rbf(mean), den = rbf(__builtin_amdgcn_sqrtf(rbf(rbf(q * (1.f / 512.f)) + 1e-5f))), rden = 1.f / den;
  u32x4 o0, o1;
#pragma unroll
  for (int i = 0; i < 4; ++i) {
    o0[i] = pk(rbf(v[2 * i] - mb) * rden, rbf(v[2 * i + 1] - mb) * rden);
    o1[i] = pk(rbf(v[8 + 2 * i] - mb) * rden, rbf(v[9 + 2 * i] - mb) * rden);
  }
  *(LAS u32x4*)(dst) = o0; *(LAS u32x4*)(dst + 16) = o1;
}

extern "C" __global__ void __launch_bounds__(320, 2) fused_decode(const void* x, const void* kc, const void* vc, const void* wq, void* ws, const void* wo,
                                                                  const void* wg, const void* wu, const void* wd, void* out) {
  SB();
  __shared__ __attribute__((aligned(16))) unsigned char smem[L_END];
  LAS unsigned char* L = (LAS unsigned char*)smem;
#define LDS32(off) (*(volatile LAS unsigned*)(L + (off)))
#define LDS64(off) (*(volatile LAS f32x2*)(L + (off)))
#define LDS128(off) (*(volatile LAS u32x4*)(L + (off)))
  const int lane = threadIdx.x & 63, w = __builtin_amdgcn_readfirstlane(threadIdx.x >> 6);
  const int bid = blockIdx.x;
  const int h = (bid & 7) * 4 + (bid >> 6), j = (bid >> 3) & 7;
  const int g = lane >> 4, n = lane & 15;
  constexpr float LOG2E = 1.4426950408889634f;
  unsigned long long cbeg; unsigned clen;
  asm volatile("s_getpc_b64 %0\nfd_code_beg:\n\ts_mov_b32 %1, fd_code_end-fd_code_beg" : "=s"(cbeg), "=s"(clen));
  const unsigned cpf = __builtin_amdgcn_raw_buffer_load_b32(mk((const void*)cbeg, (int)clen), (w == NSW - 1 && lane < 8) ? ((bid >> 3) * 8 + lane) * 128 : OOB, 0, 0);
  size_t kvbase = ((size_t)((j >> 2) * 32 + h) * 4096 + (j & 3) * 1024) * 256;
  int voff = w < NSW ? w * 4096 + lane * 16 : OOB, rot = j + 8 * (bid >> 6);
  int xoff = ((lane >> 2) & 1) * 1024 + ((lane & 3) | ((lane >> 3) << 2)) * 32;
  int so0 = (rot & 15) * 16384, so1 = ((rot + 1) & 15) * 16384;
  asm volatile("" : "+s"(kvbase), "+v"(voff), "+v"(xoff), "+s"(so0), "+s"(so1));
  SB();
  const rsrc_t rk = mk((const char*)kc + kvbase, 256 * 1024);
  u32x4 K[NL], V[NL];
#pragma unroll
  for (int i = 0; i <= BARI; ++i) { K[i] = ld16<AUX_NT>(rk, voff + (i & 3) * 1024, i < 4 ? so0 : so1); SB(); }
  SB();
  const rsrc_t rx = mk(x, 2048), rw = mk(ws, WS_END);
  u32x4 xa, xb, W[16];
  unsigned ep;
  if (w == NSW) {
    __builtin_amdgcn_s_setprio(3);
    xa = ld16<0>(rx, xoff, 0); xb = ld16<0>(rx, xoff + 16, 0);
    ep = __builtin_amdgcn_raw_buffer_load_b32(rw, WS_EPOCH, 0, AUX_SC1);
    SB();
    tile_load<8192>(W, mk((const char*)wq + (size_t)j * 64 * 8192 + h * 256, 64 * 8192), g * 8 * 8192 + n * 16);
    SB();
    if (lane < 20) LDS32(L_ML + lane * 4) = 0u;
    lgkm0();
  }
  __builtin_amdgcn_s_barrier();
  SB();
  int w2 = w;
  asm volatile("" : "+s"(w2));
  if (__builtin_expect(w2 < NSW, 1)) {
    const rsrc_t rv = mk((const char*)vc + kvbase, 256 * 1024);
#define ISSUE(i) do { if constexpr ((i) < NL) K[(i) % NL] = ld16<AUX_NT>(rk, voff + ((i) & 3) * 1024, ((((i) >> 2) + rot) & 15) * 16384); \
                      else V[(i) % NL] = ld16<AUX_NT>(rv, voff + ((i) & 3) * 1024, ((((i) >> 2) + rot) & 15) * 16384); } while (0)
    static_for<PREQ - BARI - 1>([&](auto I) {
      constexpr int i = I.value + BARI + 1, win = (WPREQ + (i & 3)) / 4;
      if constexpr (i >= win) vmwait<win - 1>();
      if constexpr (i == NCK) LDS32(L_T) = now() | 1u;
      ISSUE(i); SB();
    });
    asm volatile("" ::"v"(cpf));
    u32x4 q;
    unsigned stag;
    const int toff = (w == 0 && bid < 64 && lane == 0) ? WS_T + bid * 16 : OOB;
    for (unsigned spin = 0; spin < SPIN_CAP * 128; ++spin) {
      const unsigned f = LDS32(L_QF);
      q = LDS128(L_Q + n * 16);
      stag = LDS32(L_TAG);
      if (__builtin_amdgcn_readfirstlane(f) != 0u) break;
      __builtin_amdgcn_s_sleep(2);
    }
    SB();
    unsigned long long wtab, wtgt = 0;
    asm volatile("s_getpc_b64 %0\n\ts_branch 9f\n\t.irp k,0,1,2,3,4,5,6,7,8,9\n\ts_waitcnt vmcnt(%1+\\k)\n\ts_setpc_b64 vcc\n\t.endr\n9:" : "=s"(wtab) : "n"(WMIN - 1));
    wtab += 4;
    float xs[16], ys[8], sc[NG], p[NG], pl[NG], acc[8];
    int oaddr = L_O + w * 512 + (8 * n + 2 * g) * 4, faddr = L_OF + w * 4;
    asm volatile("" : "+v"(oaddr), "+v"(faddr));
#pragma unroll
    for (int k = 0; k < 8; ++k) acc[k] = 0.f;
    static_for<SCH.nstep>([&](auto T) {
      constexpr int t = T.value, i0 = PREQ + t;
      if constexpr (i0 < 2 * NL) {
        if constexpr ((t & 3) == 0) {
          const unsigned org = __builtin_amdgcn_readfirstlane(LDS32(L_QF));
          unsigned lev8;
          asm volatile("s_memrealtime vcc\n\ts_waitcnt lgkmcnt(0)\n\ts_sub_u32 vcc_lo, vcc_lo, %1\n\ts_sub_i32 vcc_lo, vcc_lo, %2\n\ts_ashr_i32 vcc_lo, vcc_lo, %3\n\t"
                       "s_max_i32 vcc_lo, vcc_lo, 0\n\ts_min_i32 vcc_lo, vcc_lo, %4\n\ts_lshl_b32 %0, vcc_lo, 3"
                       : "=s"(lev8) : "s"(org), "n"(SCHD + (i0 - NCK) * SCHQ / 4 - ((WMID - WMIN) << (i0 >= NLE ? LSHE : LSH))), "n"(i0 >= NLE ? LSHE : LSH), "n"(WMAX - WMIN) : "vcc", "scc", "memory");
          wtgt = wtab + lev8;
        }
        asm volatile("s_swappc_b64 vcc, %0" ::"s"(wtgt) : "vcc", "memory");
        if constexpr (i0 == NCK1 || i0 == NCK2) { const unsigned tn = now() | 1u; LDS32(L_T + (i0 == NCK1 ? 4 : 8)) = tn; st_pair(rw, toff + (i0 == NCK1 ? 1024 : 2048), tn, tn, stag); }
        ISSUE(i0);
      }
      SB();
#define TR(D, S, HALF, CTRL) _Pragma("unroll") for (int u = 0; u < HALF; ++u) { const float a_ = S[u] + dpp<CTRL>(S[u]), b_ = S[u + HALF] + dpp<CTRL>(S[u + HALF]); D[u] = (n & HALF) ? b_ : a_; }
      if constexpr (SCH.fin[t] >= 0) {
        TR(ys, ys, 4, XOR7) TR(ys, ys, 2, XOR2) TR(ys, ys, 1, XOR1)
        sc[SCH.fin[t]] = rbf(rbf(ys[0]) * 0.08838834764831845f);
      }
      static_for<SCH.kend[t] - SCH.kbeg[t]>([&](auto U) {
        constexpr int i = SCH.kbeg[t] + U.value;
        float d = dot2(K[i][0], q[0], 0.f); d = dot2(K[i][1], q[1], d); d = dot2(K[i][2], q[2], d); d = dot2(K[i][3], q[3], d);
        xs[i & 15] = d;
        if constexpr ((i & 15) == 15) { TR(ys, xs, 8, ROR8) }
      });
#undef TR
      if constexpr (t == SCH.smstep) {
        const float m = wave_max(fmaxf(fmaxf(sc[0], sc[1]), fmaxf(sc[2], sc[3])));
        float l = 0.f;
#pragma unroll
        for (int u = 0; u < NG; ++u) { p[u] = __builtin_amdgcn_exp2f((sc[u] - m) * LOG2E); l += p[u]; p[u] = rbf(p[u]); pl[u] = __uint_as_float(__float_as_uint(p[u]) >> 16); }
        LDS64(L_ML + w * 8) = f32x2{m, wave_sum(l)};
      }
      static_for<SCH.vend[t] - SCH.vbeg[t]>([&](auto U) {
        constexpr int i = SCH.vbeg[t] + U.value;
        const unsigned ph = __float_as_uint(dpp<BCAST + (i & 15)>(p[i >> 4])), pw = __float_as_uint(dpp<BCAST + (i & 15)>(pl[i >> 4]));
#pragma unroll
        for (int k = 0; k < 4; ++k) { acc[2 * k] = dot2(V[i][k], pw, acc[2 * k]); acc[2 * k + 1] = dot2(V[i][k], ph, acc[2 * k + 1]); }
        if constexpr (i == NL - 1 - KL) { SB(); LDS32(faddr + (L_IF - L_OF)) = 1u; }
      });
      SB();
    });
#undef ISSUE
    const float t0 = sw32_add(acc[0], acc[4]), t1 = sw32_add(acc[1], acc[5]), t2 = sw32_add(acc[2], acc[6]), t3 = sw32_add(acc[3], acc[7]);
    LDS64(oaddr) = f32x2{sw16_add(t0, t2), sw16_add(t1, t3)};
    lgkm0();
    __builtin_amdgcn_s_barrier();
    return;
  }

  const int eh = lane >> 5, bo = g & 1;
  layernorm(xa, xb, L + L_XN + xoff);
  const unsigned tag = __builtin_amdgcn_readfirstlane(ep) + 1u;
  const u32x4 t4 = {tag & 255u, ((tag >> 8) + tag + 0x55u) & 255u, ((tag >> 16) + tag + 0xAAu) & 255u, ((tag >> 24) + tag + 0x33u) & 255u};
  lgkm0();
  const int pub = bo * 512 + (2 * n + eh) * 16, arow = (lane >> 2) & 1;
  const u32x4 a0 = LDS128(L_XN + arow * 1024 + (64 * j + 8 * g) * 2), a1 = LDS128(L_XN + arow * 1024 + (64 * j + 8 * g) * 2 + 64);
  const u32x4 n4a = {tag & 15u, ((tag >> 4) + tag + 5u) & 15u, ((tag >> 8) + tag + 10u) & 15u, ((tag >> 12) + tag + 3u) & 15u};
  const u32x4 n4b = {((tag >> 16) + tag + 6u) & 15u, ((tag >> 20) + tag + 9u) & 15u, ((tag >> 24) + tag + 12u) & 15u, ((tag >> 28) + tag + 1u) & 15u};
  st_quad<15u>(rw, WS_Q + (h * 8 + j) * 1024 + pub, tile2(a0, a1, W, eh), eh ? n4b : n4a);
  bool alive = true;
#define POLL_END(SLP, CAP) if (spin > (CAP)) { alive = false; break; } __builtin_amdgcn_s_sleep(SLP); asm volatile("" ::: "memory");
#define GATHER(N, OFF, S, SLP, SUM, TG, TM) { u32x4 v_[N]; \
    for (unsigned spin = 0;; ++spin) { unsigned bad_ = 0u; \
      _Pragma("unroll") for (int i = 0; i < N; ++i) v_[i] = ld16<AUX_SC1>(rw, OFF, i * (S)); \
      _Pragma("unroll") for (int i = 0; i < N; ++i) { v_[i] ^= TG; bad_ |= (v_[i][0] | v_[i][1]) | (v_[i][2] | v_[i][3]); } \
      if (__all((bad_ & TM) == 0u)) break; \
      POLL_END(SLP, SPIN_CAP) } \
    SUM = __builtin_bit_cast(f32x4, v_[0]); \
    _Pragma("unroll") for (int i = 1; i < N; ++i) SUM += __builtin_bit_cast(f32x4, v_[i]); }
  {
    f32x4 qs;
    vmwait<0>();
    const u32x4 nq = (lane & 1) ? n4b : n4a;
    GATHER(4, WS_Q + (h * 8 + eh) * 1024 + (j >> 2) * 512 + (lane & 31) * 16, 2048, 2, qs, nq, 15u)
    LDS32(L_Q + (2 * (lane & 31) + eh) * 4) = pk(sw32_add(qs[0], qs[2]), sw32_add(qs[1], qs[3]));
  }
  SB();
  u32x4 W2[16];
  const int hr = (h & 3) * 8 + (h >> 2), kb = (hr >= 22 ? hr - 22 : 10) * 2 + (j & 1);
  const bool gu = hr < 22, dn = hr >= 21;
#define WD_LOADS() { const int base = kb * 64 * 1024 + (j >> 1) * 256; tile_load<1024>(W2, mk((const char*)wd + base, 1376 * 1024 - base), g * 8 * 1024 + n * 16); SB(); }
  if (gu) {
    const int cb = hr >= 11 ? hr - 11 : hr, base = (128 * (j >> 1) + (j & 1)) * 2752 + cb * 256 - (j & 1) * 64;
    const bool off = (j & 1) ? (cb == 0 && n < 4) : (cb == 10 && n >= 12);
    tile_load<5504>(W2, mk((const char*)(hr >= 11 ? wu : wg) + base, 512 * 2752 - base), off ? OOB : g * 8 * 5504 + n * 16);
  }
  SB();
  unsigned tck = 1u;
#define T_PUB(K) { for (unsigned spin = 0;; ++spin) { tck = __builtin_amdgcn_readfirstlane(LDS32(L_T + 4 * (K))); if (tck != 0u) break; POLL_END(2, SPIN_CAP * 4) } \
    if ((K) == 0 && bid < 64 && lane == 0) st_pair(rw, WS_T + (K) * 1024 + bid * 16, tck, tck, tag); }
#define T_GET(K, WAIT, SHIFT, BT) { for (unsigned spin = 0; spin < 4096u && (int)(now() - tck) < (WAIT); ++spin) __builtin_amdgcn_s_sleep(4); \
    const int tn_ = (int)(now() - tck) - 50; \
    const u32x4 tv = ld16<AUX_SC1>(rw, WS_T + (K) * 1024 + lane * 16, 0); \
    float d = fminf(fmaxf((float)(tag_ok(tv, tag) ? (int)(tv[0] - tck) : tn_), -1000.f), 1000.f); \
    d += dpp<ROR8>(d); d = sw16_add(d, d); d = sw32_add(d, d); \
    float mx = fmaxf(d, dpp<XOR7>(d)); mx = fmaxf(mx, dpp<XOR2>(mx)); mx = fmaxf(mx, dpp<XOR1>(mx)); \
    d += dpp<XOR7>(d); d += dpp<XOR2>(d); d += dpp<XOR1>(d); \
    LDS32(L_QF) = (tck + (unsigned)((int)(d * ((1.f - (BT)) / 64.f) + mx * ((BT) / 8.f)) + (SHIFT))) | 1u; }
  LDS32(L_TAG) = tag;
  T_PUB(0)
  LDS32(L_QF) = tck | 1u;
  if (gu) {
    const unsigned sel = (j & 1) ? 0x07060302u : 0x05040100u;
    u32x4 e[2];
#pragma unroll
    for (int s = 0; s < 2; ++s) {
      const u32x4 d0 = LDS128(L_XN + arow * 1024 + (128 * (j >> 1) + 64 * s + 16 * g) * 2), d1 = LDS128(L_XN + arow * 1024 + (128 * (j >> 1) + 64 * s + 16 * g) * 2 + 16);
      e[s] = u32x4{__builtin_amdgcn_perm(d0[1], d0[0], sel), __builtin_amdgcn_perm(d0[3], d0[2], sel), __builtin_amdgcn_perm(d1[1], d1[0], sel), __builtin_amdgcn_perm(d1[3], d1[2], sel)};
    }
    st_quad<255u>(rw, WS_G + (hr * 8 + j) * 1024 + pub, tile2(e[0], e[1], W2, eh), t4);
  }
  SB();
  if (dn) WD_LOADS()
  constexpr int QL = 2;
  const bool fin = bid < 256 / QL;
  const int ocol = (bid & 31) * 32 + ((bid >> 5) * QL + (lane & (QL - 1))) * 4 + g;
  const int foff = WS_Y + (bid & 31) * 8192 + (lane / QL) * 128 + ((bid >> 5) * QL + (lane & (QL - 1))) * 16, ooff2 = n < QL ? ocol * 2 : OOB;
  unsigned short xres = 0;
  if (fin) xres = __builtin_amdgcn_raw_buffer_load_b16(rx, ocol * 2, 0, 0);
  T_GET(0, TGAT, 0, BETA0)
  T_PUB(1)
  f32x2 ml;
  {
    for (unsigned spin = 0;; ++spin) {
      ml = LDS64(L_ML + (lane & 3) * 8);
      if (__all(__float_as_uint(ml[1]) != 0u)) break;
      POLL_END(4, SPIN_CAP * 4)
    }
    float M = fmaxf(ml[0], dpp<XOR2>(ml[0])); M = fmaxf(M, dpp<XOR1>(M));
    float Ls = __builtin_amdgcn_exp2f((ml[0] - M) * LOG2E) * ml[1];
    Ls += dpp<XOR2>(Ls); Ls += dpp<XOR1>(Ls);
    if (lane == 0) st_pair(rw, WS_ML + (h * 8 + j) * 128, __float_as_uint(M), __float_as_uint(Ls), tag);
  }
  T_GET(1, TGAT2, DK1 - SCHD - (NCK1 - NCK) * SCHQ / 4, BETA1)
  f32x4 zf = {0.f, 0.f, 0.f, 0.f};
  u32x4 h0, h1;
  if (dn) {
    f32x4 gs;
    const int c0 = 64 * kb + 4 * n, c1 = c0 + 32 < 1408 ? c0 + 32 : c0 + 32 - 1408;
    const int ge = WS_G + (eh * 11 + (c0 >> 7)) * 8192 + bo * 512 + (c0 & 127) * 4, go = WS_G + (eh * 11 + (c1 >> 7)) * 8192 + bo * 512 + (c1 & 127) * 4;
    GATHER(8, ((i & 1) ? go : ge), 1024, 32, gs, t4, 255u)
    const auto t = __builtin_amdgcn_permlane32_swap(pk(gs[0], gs[1]), pk(gs[2], gs[3]), false, false);
    const float g0 = lo16(t[0]), g1 = hi16(t[0]);
    constexpr float C1 = -2.f * 0.7978845608028654f * LOG2E, C3 = C1 * 0.044715f;
    const float e0 = __builtin_amdgcn_exp2f(g0 * fmaf(C3, g0 * g0, C1)), e1 = __builtin_amdgcn_exp2f(g1 * fmaf(C3, g1 * g1, C1));
    LDS32(L_H + bo * 128 + (2 * n + eh) * 4) = pk(rbf(g0 / (1.f + e0)) * lo16(t[1]), rbf(g1 / (1.f + e1)) * hi16(t[1]));
    lgkm0();
    h0 = LDS128(L_H + arow * 128 + g * 16); h1 = LDS128(L_H + arow * 128 + 64 + g * 16);
    zf = tile2(h0, h1, W2, eh);
  }
  float cw;
  {
    u32x4 v;
    for (unsigned spin = 0;; ++spin) {
      v = ld16<AUX_SC1>(rw, WS_ML + (h * 8 + (j & 4) + (lane & 3)) * 128, 0);
      if (__all(tag_ok(v, tag))) break;
      POLL_END(16, SPIN_CAP)
    }
    const float Ms = __uint_as_float(v[0]);
    float Mg = fmaxf(Ms, dpp<XOR2>(Ms)); Mg = fmaxf(Mg, dpp<XOR1>(Mg));
    float Lg = __builtin_amdgcn_exp2f((Ms - Mg) * LOG2E) * __uint_as_float(v[2]);
    Lg += dpp<XOR2>(Lg); Lg += dpp<XOR1>(Lg);
    cw = __builtin_amdgcn_exp2f((ml[0] - Mg) * LOG2E) / Lg;
  }
  if (!alive) zf[0] = __builtin_nanf("");
  T_PUB(2)
  T_GET(2, TGAT2, DK2 - SCHD - (NCK2 - NCK) * SCHQ / 4, BETA2)
  {
    const uintptr_t wop = (uintptr_t)wo + (size_t)(h * 128 + 64 * (j & 1)) * 1024 + (j >> 1) * 256;
    const u32x4 rs = {(unsigned)(uintptr_t)ws, (unsigned)((uintptr_t)ws >> 32) & 0xffffu, (unsigned)WS_END, 0x20000u};
    const u32x4 rwo = {(unsigned)wop, (unsigned)(wop >> 32) & 0xffffu, 64u * 1024u, 0x20000u};
    const u32x4 ro = {(unsigned)(uintptr_t)out, (unsigned)((uintptr_t)out >> 32) & 0xffffu, 2048u, 0x20000u};
    const int ooff = WS_O + (h * 8 + j) * 512 + lane * 8;
    const int goff = WS_O + (h * 8 + (n & 7)) * 512 + (32 * (j & 1) + 4 * g) * 8 + (n >> 3) * 128;
    const int yoff = WS_Y + ((bo * 16 + (j >> 1) * 4 + (n >> 2)) * 64 + 2 * h + (j & 1)) * 128 + ((n & 3) * 2 + eh) * 16;
    const unsigned lbase = (unsigned)(uintptr_t)L;
    unsigned sp = SPIN_CAP * 8;
    const unsigned anc = __builtin_amdgcn_readfirstlane(LDS32(L_QF));
    asm volatile(
      "s_nop 0\n\t"
      "v_readlane_b32 s64, %[cw], 0\n\t"
      "v_readlane_b32 s65, %[cw], 1\n\t"
      "v_readlane_b32 s66, %[cw], 2\n\t"
      "v_readlane_b32 s67, %[cw], 3\n\t"
      "s_mov_b32 s68, 0x00ff00ff\n\t"
      "s_mov_b32 s69, 0x00ff00ff\n\t"
      "s_movk_i32 s70, 0x1000\n\t"
      "s_mov_b32 s71, 0x8000\n\t"
      "s_mov_b32 s72, 0x9000\n\t"
      "s_mov_b32 s73, 0x05040100\n\t"
      "s_mov_b32 s74, 0x07060302\n\t"
      "s_memrealtime s[76:77]\n\t"
      "s_waitcnt lgkmcnt(0)\n\t"
      "s_sub_u32 s76, s76, %[anc]\n\t"
      "s_cmpk_gt_i32 s76, 1300\n\t"
      "s_cbranch_scc1 20f\n\t"
      "s_mov_b64 vcc, 0\n\t"
      "s_mov_b64 exec, 0\n\t"
      "20:\n\t"
      "v_mov_b32 v160, %[z0]\n\t"
      "v_mov_b32 v161, 0\n\t"
      "v_mov_b32 v162, 0\n\t"
      "v_mov_b32 v163, 0\n\t"
      "v_mov_b32 v164, %[z1]\n\t"
      "v_mov_b32 v165, 0\n\t"
      "v_mov_b32 v166, 0\n\t"
      "v_mov_b32 v167, 0\n\t"
      "v_mov_b32 v168, %[z2]\n\t"
      "v_mov_b32 v169, 0\n\t"
      "v_mov_b32 v170, 0\n\t"
      "v_mov_b32 v171, 0\n\t"
      "v_mov_b32 v172, %[z3]\n\t"
      "v_mov_b32 v173, 0\n\t"
      "v_mov_b32 v174, 0\n\t"
      "v_mov_b32 v175, 0\n\t"
      "v_mov_b32 v176, 0xffffff00\n\t"
      "v_mov_b32 v145, %[tag]\n\t"
      "v_mov_b32 v177, %[tag]\n\t"
      "s_waitcnt vmcnt(0)\n\t"
      "6:\n\t"
      "s_cbranch_execz 30f\n\t"
      "ds_read_b32 v146, %[lf] offset:16\n\t"
      "s_waitcnt lgkmcnt(0)\n\t"
      "30:\n\t"
      "v_cmp_gt_u32 vcc, 1, v146\n\t"
      "s_cbranch_vccz 7f\n\t"
      "s_sleep 1\n\t"
      "s_sub_u32 %[sp], %[sp], 1\n\t"
      "s_cmp_ge_i32 %[sp], 0\n\t"
      "s_cbranch_scc1 6b\n\t"
      "7:\n\t"
      "s_cbranch_execz 30f\n\t"
      "buffer_load_dwordx4 v[32:35], %[wv], %[rwo], 0 offen offset:0 nt\n\t"
      "buffer_load_dwordx4 v[36:39], %[wv], %[rwo], 0 offen offset:1024 nt\n\t"
      "buffer_load_dwordx4 v[40:43], %[wv], %[rwo], 0 offen offset:2048 nt\n\t"
      "buffer_load_dwordx4 v[44:47], %[wv], %[rwo], 0 offen offset:3072 nt\n\t"
      "buffer_load_dwordx4 v[48:51], %[wv], %[rwo], s70 offen offset:0 nt\n\t"
      "buffer_load_dwordx4 v[52:55], %[wv], %[rwo], s70 offen offset:1024 nt\n\t"
      "buffer_load_dwordx4 v[56:59], %[wv], %[rwo], s70 offen offset:2048 nt\n\t"
      "buffer_load_dwordx4 v[60:63], %[wv], %[rwo], s70 offen offset:3072 nt\n\t"
      "30:\n\t"
      "s_cbranch_execz 30f\n\t"
      "buffer_load_dwordx4 v[64:67], %[wv], %[rwo], s71 offen offset:0 nt\n\t"
      "buffer_load_dwordx4 v[68:71], %[wv], %[rwo], s71 offen offset:1024 nt\n\t"
      "buffer_load_dwordx4 v[72:75], %[wv], %[rwo], s71 offen offset:2048 nt\n\t"
      "buffer_load_dwordx4 v[76:79], %[wv], %[rwo], s71 offen offset:3072 nt\n\t"
      "buffer_load_dwordx4 v[80:83], %[wv], %[rwo], s72 offen offset:0 nt\n\t"
      "buffer_load_dwordx4 v[84:87], %[wv], %[rwo], s72 offen offset:1024 nt\n\t"
      "buffer_load_dwordx4 v[88:91], %[wv], %[rwo], s72 offen offset:2048 nt\n\t"
      "buffer_load_dwordx4 v[92:95], %[wv], %[rwo], s72 offen offset:3072 nt\n\t"
      "30:\n\t"
      "s_cbranch_execz 30f\n\t"
      "s_barrier\n\t"
      "ds_read_b64 v[148:149], %[lo] offset:0\n\t"
      "ds_read_b64 v[150:151], %[lo] offset:512\n\t"
      "ds_read_b64 v[152:153], %[lo] offset:1024\n\t"
      "ds_read_b64 v[154:155], %[lo] offset:1536\n\t"
      "30:\n\t"
      "s_waitcnt lgkmcnt(3)\n\t"
      "v_mul_f32 v146, s64, v148\n\t"
      "v_mul_f32 v147, s64, v149\n\t"
      "s_waitcnt lgkmcnt(2)\n\t"
      "v_fmac_f32 v146, s65, v150\n\t"
      "v_fmac_f32 v147, s65, v151\n\t"
      "s_waitcnt lgkmcnt(1)\n\t"
      "v_fmac_f32 v146, s66, v152\n\t"
      "v_fmac_f32 v147, s66, v153\n\t"
      "s_waitcnt lgkmcnt(0)\n\t"
      "v_fmac_f32 v146, s67, v154\n\t"
      "v_fmac_f32 v147, s67, v155\n\t"
      "v_cvt_pk_bf16_f32 v144, v146, v147\n\t"
      "s_cbranch_execz 30f\n\t"
      "buffer_store_dwordx2 v[144:145], %[o], %[rs], 0 offen sc1\n\t"
      "v_mov_b32 v148, 0\n\t"
      "v_mov_b32 v149, 0\n\t"
      "v_mov_b32 v150, 0\n\t"
      "v_mov_b32 v151, 0\n\t"
      "v_mov_b32 v152, 0\n\t"
      "v_mov_b32 v153, 0\n\t"
      "v_mov_b32 v154, 0\n\t"
      "v_mov_b32 v155, 0\n\t"
      "s_sleep 10\n\t"
      "14:\n\t"
      "s_memrealtime s[76:77]\n\t"
      "s_waitcnt lgkmcnt(0)\n\t"
      "s_sub_u32 s76, s76, %[anc]\n\t"
      "s_sub_u32 s76, s76, 1480\n\t"
      "s_cmpk_lt_i32 s76, -300\n\t"
      "s_cbranch_scc1 15f\n\t"
      "s_cmp_lt_i32 s76, 0\n\t"
      "s_cbranch_scc1 14b\n\t"
      "15:\n\t"
      "buffer_load_dwordx4 v[96:99], %[g], %[rs], 0 offen offset:0 sc1\n\t"
      "buffer_load_dwordx4 v[100:103], %[g], %[rs], 0 offen offset:16 sc1\n\t"
      "30:\n\t"
      "s_waitcnt vmcnt(17)\n\t"
      "v_perm_b32 v0, v36, v32, s73\n\t"
      "v_perm_b32 v4, v36, v32, s74\n\t"
      "v_perm_b32 v8, v37, v33, s73\n\t"
      "v_perm_b32 v12, v37, v33, s74\n\t"
      "v_perm_b32 v16, v38, v34, s73\n\t"
      "v_perm_b32 v20, v38, v34, s74\n\t"
      "v_perm_b32 v24, v39, v35, s73\n\t"
      "v_perm_b32 v28, v39, v35, s74\n\t"
      "s_waitcnt vmcnt(15)\n\t"
      "v_perm_b32 v1, v44, v40, s73\n\t"
      "v_perm_b32 v5, v44, v40, s74\n\t"
      "v_perm_b32 v9, v45, v41, s73\n\t"
      "v_perm_b32 v13, v45, v41, s74\n\t"
      "v_perm_b32 v17, v46, v42, s73\n\t"
      "v_perm_b32 v21, v46, v42, s74\n\t"
      "v_perm_b32 v25, v47, v43, s73\n\t"
      "v_perm_b32 v29, v47, v43, s74\n\t"
      "s_waitcnt vmcnt(13)\n\t"
      "v_perm_b32 v2, v52, v48, s73\n\t"
      "v_perm_b32 v6, v52, v48, s74\n\t"
      "v_perm_b32 v10, v53, v49, s73\n\t"
      "v_perm_b32 v14, v53, v49, s74\n\t"
      "v_perm_b32 v18, v54, v50, s73\n\t"
      "v_perm_b32 v22, v54, v50, s74\n\t"
      "v_perm_b32 v26, v55, v51, s73\n\t"
      "v_perm_b32 v30, v55, v51, s74\n\t"
      "s_waitcnt vmcnt(11)\n\t"
      "v_perm_b32 v3, v60, v56, s73\n\t"
      "v_perm_b32 v7, v60, v56, s74\n\t"
      "v_perm_b32 v11, v61, v57, s73\n\t"
      "v_perm_b32 v15, v61, v57, s74\n\t"
      "v_perm_b32 v19, v62, v58, s73\n\t"
      "v_perm_b32 v23, v62, v58, s74\n\t"
      "v_perm_b32 v27, v63, v59, s73\n\t"
      "v_perm_b32 v31, v63, v59, s74\n\t"
      "s_cbranch_execz 30f\n\t"
      "buffer_load_dwordx4 v[112:115], %[g], %[rs], 0 offen offset:0 sc1\n\t"
      "buffer_load_dwordx4 v[116:119], %[g], %[rs], 0 offen offset:16 sc1\n\t"
      "30:\n\t"
      "s_waitcnt vmcnt(11)\n\t"
      "v_perm_b32 v32, v68, v64, s73\n\t"
      "v_perm_b32 v36, v68, v64, s74\n\t"
      "v_perm_b32 v40, v69, v65, s73\n\t"
      "v_perm_b32 v44, v69, v65, s74\n\t"
      "v_perm_b32 v48, v70, v66, s73\n\t"
      "v_perm_b32 v52, v70, v66, s74\n\t"
      "v_perm_b32 v56, v71, v67, s73\n\t"
      "v_perm_b32 v60, v71, v67, s74\n\t"
      "s_waitcnt vmcnt(9)\n\t"
      "v_perm_b32 v33, v76, v72, s73\n\t"
      "v_perm_b32 v37, v76, v72, s74\n\t"
      "v_perm_b32 v41, v77, v73, s73\n\t"
      "v_perm_b32 v45, v77, v73, s74\n\t"
      "v_perm_b32 v49, v78, v74, s73\n\t"
      "v_perm_b32 v53, v78, v74, s74\n\t"
      "v_perm_b32 v57, v79, v75, s73\n\t"
      "v_perm_b32 v61, v79, v75, s74\n\t"
      "s_waitcnt vmcnt(7)\n\t"
      "v_perm_b32 v34, v84, v80, s73\n\t"
      "v_perm_b32 v38, v84, v80, s74\n\t"
      "v_perm_b32 v42, v85, v81, s73\n\t"
      "v_perm_b32 v46, v85, v81, s74\n\t"
      "v_perm_b32 v50, v86, v82, s73\n\t"
      "v_perm_b32 v54, v86, v82, s74\n\t"
      "v_perm_b32 v58, v87, v83, s73\n\t"
      "v_perm_b32 v62, v87, v83, s74\n\t"
      "s_waitcnt vmcnt(5)\n\t"
      "v_perm_b32 v35, v92, v88, s73\n\t"
      "v_perm_b32 v39, v92, v88, s74\n\t"
      "v_perm_b32 v43, v93, v89, s73\n\t"
      "v_perm_b32 v47, v93, v89, s74\n\t"
      "v_perm_b32 v51, v94, v90, s73\n\t"
      "v_perm_b32 v55, v94, v90, s74\n\t"
      "v_perm_b32 v59, v95, v91, s73\n\t"
      "v_perm_b32 v63, v95, v91, s74\n\t"
      "1:\n\t"
      "s_cbranch_execz 30f\n\t"
      "buffer_load_dwordx4 v[128:131], %[g], %[rs], 0 offen offset:0 sc1\n\t"
      "buffer_load_dwordx4 v[132:135], %[g], %[rs], 0 offen offset:16 sc1\n\t"
      "30:\n\t"
      "s_waitcnt vmcnt(4)\n\t"
      "v_xor_b32 v144, %[tag], v97\n\t"
      "v_bitop3_b32 v144, v144, v99, %[tag] bitop3:0xf6\n\t"
      "v_bitop3_b32 v144, v144, v101, %[tag] bitop3:0xf6\n\t"
      "v_bitop3_b32 v144, v144, v103, %[tag] bitop3:0xf6\n\t"
      "v_cmp_ne_u32 vcc, 0, v144\n\t"
      "s_cbranch_vccz 2f\n\t"
      "s_sleep 1\n\t"
      "buffer_load_dwordx4 v[96:99], %[g], %[rs], 0 offen offset:0 sc1\n\t"
      "buffer_load_dwordx4 v[100:103], %[g], %[rs], 0 offen offset:16 sc1\n\t"
      "s_waitcnt vmcnt(4)\n\t"
      "v_xor_b32 v144, %[tag], v113\n\t"
      "v_bitop3_b32 v144, v144, v115, %[tag] bitop3:0xf6\n\t"
      "v_bitop3_b32 v144, v144, v117, %[tag] bitop3:0xf6\n\t"
      "v_bitop3_b32 v144, v144, v119, %[tag] bitop3:0xf6\n\t"
      "v_cmp_ne_u32 vcc, 0, v144\n\t"
      "s_cbranch_vccz 3f\n\t"
      "s_sleep 1\n\t"
      "buffer_load_dwordx4 v[112:115], %[g], %[rs], 0 offen offset:0 sc1\n\t"
      "buffer_load_dwordx4 v[116:119], %[g], %[rs], 0 offen offset:16 sc1\n\t"
      "s_waitcnt vmcnt(4)\n\t"
      "v_xor_b32 v144, %[tag], v129\n\t"
      "v_bitop3_b32 v144, v144, v131, %[tag] bitop3:0xf6\n\t"
      "v_bitop3_b32 v144, v144, v133, %[tag] bitop3:0xf6\n\t"
      "v_bitop3_b32 v144, v144, v135, %[tag] bitop3:0xf6\n\t"
      "v_cmp_ne_u32 vcc, 0, v144\n\t"
      "s_cbranch_vccz 4f\n\t"
      "s_sleep 1\n\t"
      "s_sub_u32 %[sp], %[sp], 8\n\t"
      "s_cmp_ge_i32 %[sp], 0\n\t"
      "s_cbranch_scc1 1b\n\t"
      "4:\n\t"
      "v_cndmask_b32_e64 v144, 0, v128, s[68:69]\n\t"
      "v_cndmask_b32_e64 v145, 0, v130, s[68:69]\n\t"
      "v_cndmask_b32_e64 v146, 0, v132, s[68:69]\n\t"
      "v_cndmask_b32_e64 v147, 0, v134, s[68:69]\n\t"
      "v_mov_b32_dpp v152, v128 row_ror:8 row_mask:0xf bank_mask:0xc\n\t"
      "v_mov_b32_dpp v153, v130 row_ror:8 row_mask:0xf bank_mask:0xc\n\t"
      "v_mfma_f32_16x16x32_bf16 v[160:163], v[144:147], v[0:3], v[160:163]\n\t"
      "v_mov_b32_dpp v154, v132 row_ror:8 row_mask:0xf bank_mask:0xc\n\t"
      "v_mov_b32_dpp v155, v134 row_ror:8 row_mask:0xf bank_mask:0xc\n\t"
      "v_mov_b32_dpp v148, v128 row_ror:8 row_mask:0xf bank_mask:0x3\n\t"
      "v_mfma_f32_16x16x32_bf16 v[164:167], v[144:147], v[4:7], v[164:167]\n\t"
      "v_mov_b32_dpp v149, v130 row_ror:8 row_mask:0xf bank_mask:0x3\n\t"
      "v_mov_b32_dpp v150, v132 row_ror:8 row_mask:0xf bank_mask:0x3\n\t"
      "v_mov_b32_dpp v151, v134 row_ror:8 row_mask:0xf bank_mask:0x3\n\t"
      "v_mfma_f32_16x16x32_bf16 v[168:171], v[144:147], v[8:11], v[168:171]\n\t"
      "v_cndmask_b32_e64 v156, v128, 0, s[68:69]\n\t"
      "v_cndmask_b32_e64 v157, v130, 0, s[68:69]\n\t"
      "v_mfma_f32_16x16x32_bf16 v[172:175], v[144:147], v[12:15], v[172:175]\n\t"
      "v_cndmask_b32_e64 v158, v132, 0, s[68:69]\n\t"
      "v_cndmask_b32_e64 v159, v134, 0, s[68:69]\n\t"
      "s_branch 5f\n\t"
      "3:\n\t"
      "v_cndmask_b32_e64 v144, 0, v112, s[68:69]\n\t"
      "v_cndmask_b32_e64 v145, 0, v114, s[68:69]\n\t"
      "v_cndmask_b32_e64 v146, 0, v116, s[68:69]\n\t"
      "v_cndmask_b32_e64 v147, 0, v118, s[68:69]\n\t"
      "v_mov_b32_dpp v152, v112 row_ror:8 row_mask:0xf bank_mask:0xc\n\t"
      "v_mov_b32_dpp v153, v114 row_ror:8 row_mask:0xf bank_mask:0xc\n\t"
      "v_mfma_f32_16x16x32_bf16 v[160:163], v[144:147], v[0:3], v[160:163]\n\t"
      "v_mov_b32_dpp v154, v116 row_ror:8 row_mask:0xf bank_mask:0xc\n\t"
      "v_mov_b32_dpp v155, v118 row_ror:8 row_mask:0xf bank_mask:0xc\n\t"
      "v_mov_b32_dpp v148, v112 row_ror:8 row_mask:0xf bank_mask:0x3\n\t"
      "v_mfma_f32_16x16x32_bf16 v[164:167], v[144:147], v[4:7], v[164:167]\n\t"
      "v_mov_b32_dpp v149, v114 row_ror:8 row_mask:0xf bank_mask:0x3\n\t"
      "v_mov_b32_dpp v150, v116 row_ror:8 row_mask:0xf bank_mask:0x3\n\t"
      "v_mov_b32_dpp v151, v118 row_ror:8 row_mask:0xf bank_mask:0x3\n\t"
      "v_mfma_f32_16x16x32_bf16 v[168:171], v[144:147], v[8:11], v[168:171]\n\t"
      "v_cndmask_b32_e64 v156, v112, 0, s[68:69]\n\t"
      "v_cndmask_b32_e64 v157, v114, 0, s[68:69]\n\t"
      "v_mfma_f32_16x16x32_bf16 v[172:175], v[144:147], v[12:15], v[172:175]\n\t"
      "v_cndmask_b32_e64 v158, v116, 0, s[68:69]\n\t"
      "v_cndmask_b32_e64 v159, v118, 0, s[68:69]\n\t"
      "s_branch 5f\n\t"
      "2:\n\t"
      "v_cndmask_b32_e64 v144, 0, v96, s[68:69]\n\t"
      "v_cndmask_b32_e64 v145, 0, v98, s[68:69]\n\t"
      "v_cndmask_b32_e64 v146, 0, v100, s[68:69]\n\t"
      "v_cndmask_b32_e64 v147, 0, v102, s[68:69]\n\t"
      "v_mov_b32_dpp v152, v96 row_ror:8 row_mask:0xf bank_mask:0xc\n\t"
      "v_mov_b32_dpp v153, v98 row_ror:8 row_mask:0xf bank_mask:0xc\n\t"
      "v_mfma_f32_16x16x32_bf16 v[160:163], v[144:147], v[0:3], v[160:163]\n\t"
      "v_mov_b32_dpp v154, v100 row_ror:8 row_mask:0xf bank_mask:0xc\n\t"
      "v_mov_b32_dpp v155, v102 row_ror:8 row_mask:0xf bank_mask:0xc\n\t"
      "v_mov_b32_dpp v148, v96 row_ror:8 row_mask:0xf bank_mask:0x3\n\t"
      "v_mfma_f32_16x16x32_bf16 v[164:167], v[144:147], v[4:7], v[164:167]\n\t"
      "v_mov_b32_dpp v149, v98 row_ror:8 row_mask:0xf bank_mask:0x3\n\t"
      "v_mov_b32_dpp v150, v100 row_ror:8 row_mask:0xf bank_mask:0x3\n\t"
      "v_mov_b32_dpp v151, v102 row_ror:8 row_mask:0xf bank_mask:0x3\n\t"
      "v_mfma_f32_16x16x32_bf16 v[168:171], v[144:147], v[8:11], v[168:171]\n\t"
      "v_cndmask_b32_e64 v156, v96, 0, s[68:69]\n\t"
      "v_cndmask_b32_e64 v157, v98, 0, s[68:69]\n\t"
      "v_mfma_f32_16x16x32_bf16 v[172:175], v[144:147], v[12:15], v[172:175]\n\t"
      "v_cndmask_b32_e64 v158, v100, 0, s[68:69]\n\t"
      "v_cndmask_b32_e64 v159, v102, 0, s[68:69]\n\t"
      "5:\n\t"
      "v_mfma_f32_16x16x32_bf16 v[160:163], v[152:155], v[16:19], v[160:163]\n\t"
      "v_mfma_f32_16x16x32_bf16 v[160:163], v[148:151], v[32:35], v[160:163]\n\t"
      "v_mfma_f32_16x16x32_bf16 v[160:163], v[156:159], v[48:51], v[160:163]\n\t"
      "v_mfma_f32_16x16x32_bf16 v[164:167], v[152:155], v[20:23], v[164:167]\n\t"
      "s_nop 1\n\t"
      "v_mfma_f32_16x16x32_bf16 v[164:167], v[148:151], v[36:39], v[164:167]\n\t"
      "s_nop 1\n\t"
      "v_mfma_f32_16x16x32_bf16 v[164:167], v[156:159], v[52:55], v[164:167]\n\t"
      "s_nop 1\n\t"
      "v_mfma_f32_16x16x32_bf16 v[168:171], v[152:155], v[24:27], v[168:171]\n\t"
      "v_pk_add_f32 v[160:161], v[160:161], v[162:163]\n\t"
      "v_add_f32 v160, v160, v161\n\t"
      "v_mfma_f32_16x16x32_bf16 v[168:171], v[148:151], v[40:43], v[168:171]\n\t"
      "v_and_or_b32 v144, v160, v176, %[t0]\n\t"
      "s_nop 0\n\t"
      "v_mfma_f32_16x16x32_bf16 v[168:171], v[156:159], v[56:59], v[168:171]\n\t"
      "s_nop 1\n\t"
      "v_mfma_f32_16x16x32_bf16 v[172:175], v[152:155], v[28:31], v[172:175]\n\t"
      "v_pk_add_f32 v[164:165], v[164:165], v[166:167]\n\t"
      "v_add_f32 v164, v164, v165\n\t"
      "v_mfma_f32_16x16x32_bf16 v[172:175], v[148:151], v[44:47], v[172:175]\n\t"
      "v_and_or_b32 v145, v164, v176, %[t1]\n\t"
      "s_nop 0\n\t"
      "v_mfma_f32_16x16x32_bf16 v[172:175], v[156:159], v[60:63], v[172:175]\n\t"
      "v_pk_add_f32 v[168:169], v[168:169], v[170:171]\n\t"
      "v_add_f32 v168, v168, v169\n\t"
      "v_and_or_b32 v146, v168, v176, %[t2]\n\t"
      "s_nop 4\n\t"
      "v_pk_add_f32 v[172:173], v[172:173], v[174:175]\n\t"
      "v_add_f32 v172, v172, v173\n\t"
      "v_and_or_b32 v147, v172, v176, %[t3]\n\t"
      "s_cbranch_execz 30f\n\t"
      "buffer_store_dwordx4 v[144:147], %[y], %[rs], 0 offen sc1\n\t"
      "30:\n\t"
      "s_cmpk_ge_u32 %[bid], 128\n\t"
      "s_cbranch_scc1 12f\n\t"
      "s_cbranch_execz 30f\n\t"
      "s_waitcnt vmcnt(0)\n\t"
      "14:\n\t"
      "s_memrealtime s[76:77]\n\t"
      "s_waitcnt lgkmcnt(0)\n\t"
      "s_sub_u32 s76, s76, %[anc]\n\t"
      "s_sub_u32 s76, s76, 1545\n\t"
      "s_cmpk_lt_i32 s76, -300\n\t"
      "s_cbranch_scc1 15f\n\t"
      "s_cmp_lt_i32 s76, 0\n\t"
      "s_cbranch_scc1 14b\n\t"
      "15:\n\t"
      "buffer_load_dwordx4 v[0:3], %[f], %[rs], 0 offen offset:0 sc1\n\t"
      "buffer_load_dwordx4 v[4:7], %[f], %[rs], s70 offen offset:0 sc1\n\t"
      "s_sleep 2\n\t"
      "buffer_load_dwordx4 v[8:11], %[f], %[rs], 0 offen offset:0 sc1\n\t"
      "buffer_load_dwordx4 v[12:15], %[f], %[rs], s70 offen offset:0 sc1\n\t"
      "s_sleep 2\n\t"
      "30:\n\t"
      "10:\n\t"
      "s_cbranch_execz 30f\n\t"
      "buffer_load_dwordx4 v[16:19], %[f], %[rs], 0 offen offset:0 sc1\n\t"
      "buffer_load_dwordx4 v[20:23], %[f], %[rs], s70 offen offset:0 sc1\n\t"
      "30:\n\t"
      "s_waitcnt vmcnt(5)\n\t"
      "v_xor_b32 v0, %[t0], v0\n\t"
      "v_xor_b32 v1, %[t1], v1\n\t"
      "v_xor_b32 v2, %[t2], v2\n\t"
      "v_xor_b32 v3, %[t3], v3\n\t"
      "v_or3_b32 v148, v0, v1, v2\n\t"
      "v_or_b32 v148, v148, v3\n\t"
      "s_waitcnt vmcnt(4)\n\t"
      "v_xor_b32 v4, %[t0], v4\n\t"
      "v_xor_b32 v5, %[t1], v5\n\t"
      "v_xor_b32 v6, %[t2], v6\n\t"
      "v_xor_b32 v7, %[t3], v7\n\t"
      "v_or3_b32 v148, v148, v4, v5\n\t"
      "v_or3_b32 v148, v148, v6, v7\n\t"
      "v_pk_add_f32 v[150:151], v[0:1], v[4:5]\n\t"
      "v_pk_add_f32 v[152:153], v[2:3], v[6:7]\n\t"
      "v_and_b32 v148, 0xff, v148\n\t"
      "v_cmp_ne_u32 vcc, 0, v148\n\t"
      "s_cbranch_vccz 11f\n\t"
      "s_sleep 1\n\t"
      "buffer_load_dwordx4 v[0:3], %[f], %[rs], 0 offen offset:0 sc1\n\t"
      "buffer_load_dwordx4 v[4:7], %[f], %[rs], s70 offen offset:0 sc1\n\t"
      "s_waitcnt vmcnt(5)\n\t"
      "v_xor_b32 v8, %[t0], v8\n\t"
      "v_xor_b32 v9, %[t1], v9\n\t"
      "v_xor_b32 v10, %[t2], v10\n\t"
      "v_xor_b32 v11, %[t3], v11\n\t"
      "v_or3_b32 v148, v8, v9, v10\n\t"
      "v_or_b32 v148, v148, v11\n\t"
      "s_waitcnt vmcnt(4)\n\t"
      "v_xor_b32 v12, %[t0], v12\n\t"
      "v_xor_b32 v13, %[t1], v13\n\t"
      "v_xor_b32 v14, %[t2], v14\n\t"
      "v_xor_b32 v15, %[t3], v15\n\t"
      "v_or3_b32 v148, v148, v12, v13\n\t"
      "v_or3_b32 v148, v148, v14, v15\n\t"
      "v_pk_add_f32 v[150:151], v[8:9], v[12:13]\n\t"
      "v_pk_add_f32 v[152:153], v[10:11], v[14:15]\n\t"
      "v_and_b32 v148, 0xff, v148\n\t"
      "v_cmp_ne_u32 vcc, 0, v148\n\t"
      "s_cbranch_vccz 11f\n\t"
      "s_sleep 1\n\t"
      "buffer_load_dwordx4 v[8:11], %[f], %[rs], 0 offen offset:0 sc1\n\t"
      "buffer_load_dwordx4 v[12:15], %[f], %[rs], s70 offen offset:0 sc1\n\t"
      "s_waitcnt vmcnt(5)\n\t"
      "v_xor_b32 v16, %[t0], v16\n\t"
      "v_xor_b32 v17, %[t1], v17\n\t"
      "v_xor_b32 v18, %[t2], v18\n\t"
      "v_xor_b32 v19, %[t3], v19\n\t"
      "v_or3_b32 v148, v16, v17, v18\n\t"
      "v_or_b32 v148, v148, v19\n\t"
      "s_waitcnt vmcnt(4)\n\t"
      "v_xor_b32 v20, %[t0], v20\n\t"
      "v_xor_b32 v21, %[t1], v21\n\t"
      "v_xor_b32 v22, %[t2], v22\n\t"
      "v_xor_b32 v23, %[t3], v23\n\t"
      "v_or3_b32 v148, v148, v20, v21\n\t"
      "v_or3_b32 v148, v148, v22, v23\n\t"
      "v_pk_add_f32 v[150:151], v[16:17], v[20:21]\n\t"
      "v_pk_add_f32 v[152:153], v[18:19], v[22:23]\n\t"
      "v_and_b32 v148, 0xff, v148\n\t"
      "v_cmp_ne_u32 vcc, 0, v148\n\t"
      "s_cbranch_vccz 11f\n\t"
      "s_sleep 1\n\t"
      "s_sub_u32 %[sp], %[sp], 8\n\t"
      "s_cmp_ge_i32 %[sp], 0\n\t"
      "s_cbranch_scc1 10b\n\t"
      "11:\n\t"
      "v_permlane32_swap_b32 v150, v152\n\t"
      "v_permlane32_swap_b32 v151, v153\n\t"
      "v_add_f32 v150, v150, v152\n\t"
      "v_add_f32 v151, v151, v153\n\t"
      "s_nop 1\n\t"
      "v_permlane16_swap_b32 v150, v151\n\t"
      "v_add_f32 v150, v150, v151\n\t"
      "s_nop 1\n\t"
      "v_add_f32_dpp v150, v150, v150 row_ror:8 row_mask:0xf bank_mask:0xf\n\t"
      "s_nop 1\n\t"
      "v_add_f32_dpp v150, v150, v150 row_ror:4 row_mask:0xf bank_mask:0xf\n\t"
      "s_nop 1\n\t"
      "v_add_f32_dpp v150, v150, v150 row_ror:2 row_mask:0xf bank_mask:0xf\n\t"
      "v_add_f32 v150, v150, %[x]\n\t"
      "v_cvt_pk_bf16_f32 v150, v150, v150\n\t"
      "s_cbranch_execz 30f\n\t"
      "buffer_store_short v150, %[o2], %[ro], 0 offen\n\t"
      "buffer_store_dword v177, %[e], %[rs], 0 offen\n\t"
      "30:\n\t"
      "12:\n\t"
      "s_cbranch_execnz 22f\n\t"
      "s_mov_b64 exec, -1\n\t"
      "s_branch 20b\n\t"
      "22:\n\t"
      "s_waitcnt vmcnt(0) lgkmcnt(0)\n\t"
      : [sp] "+&s"(sp)
      : [g] "v"(goff), [y] "v"(yoff), [o] "v"(ooff), [wv] "v"(g * 8 * 1024 + n * 16), [lf] "v"(lbase + L_OF + (lane & 3) * 4), [lo] "v"(lbase + L_O + lane * 8),
        [f] "v"(foff), [o2] "v"(ooff2), [x] "v"(lo16(xres)), [e] "v"((bid == 0 && lane == 0) ? WS_EPOCH : OOB), [cw] "v"(cw),
        [z0] "v"(zf[0]), [z1] "v"(zf[1]), [z2] "v"(zf[2]), [z3] "v"(zf[3]),
        [rs] "s"(rs), [rwo] "s"(rwo), [ro] "s"(ro), [tag] "s"(tag), [bid] "s"(bid), [anc] "s"(anc), [t0] "s"(t4[0]), [t1] "s"(t4[1]), [t2] "s"(t4[2]), [t3] "s"(t4[3])
      : "vcc", "scc", "memory", "s64", "s65", "s66", "s67", "s68", "s69", "s70", "s71", "s72", "s73", "s74", "s75", "s76", "s77", "v0", "v1", "v2", "v3", "v4", "v5", "v6", "v7", "v8", "v9", "v10", "v11", "v12", "v13", "v14", "v15", "v16", "v17", "v18", "v19", "v20", "v21", "v22", "v23", "v24", "v25", "v26", "v27", "v28", "v29", "v30", "v31", "v32", "v33", "v34", "v35", "v36", "v37", "v38", "v39", "v40", "v41", "v42", "v43", "v44", "v45", "v46", "v47", "v48", "v49", "v50", "v51", "v52", "v53", "v54", "v55", "v56", "v57", "v58", "v59", "v60", "v61", "v62", "v63", "v64", "v65", "v66", "v67", "v68", "v69", "v70", "v71", "v72", "v73", "v74", "v75", "v76", "v77", "v78", "v79", "v80", "v81", "v82", "v83", "v84", "v85", "v86", "v87", "v88", "v89", "v90", "v91", "v92", "v93", "v94", "v95", "v96", "v97", "v98", "v99", "v100", "v101", "v102", "v103", "v104", "v105", "v106", "v107", "v108", "v109", "v110", "v111", "v112", "v113", "v114", "v115", "v116", "v117", "v118", "v119", "v120", "v121", "v122", "v123", "v124", "v125", "v126", "v127", "v128", "v129", "v130", "v131", "v132", "v133", "v134", "v135", "v136", "v137", "v138", "v139", "v140", "v141", "v142", "v143", "v144", "v145", "v146", "v147", "v148", "v149", "v150", "v151", "v152", "v153", "v154", "v155", "v156", "v157", "v158", "v159", "v160", "v161", "v162", "v163", "v164", "v165", "v166", "v167", "v168", "v169", "v170", "v171", "v172", "v173", "v174", "v175", "v176", "v177");
    if (fin && (!alive || (int)sp < 0)) __builtin_amdgcn_raw_buffer_store_b16((unsigned short)0x7fc0u, mk(out, 2048), ooff2, 0, 0);
  }
  asm volatile("fd_code_end:");
}

extern "C" void kernel_launch(void* const* d_in, const int* in_sizes, int n_in, void* d_out, int out_size, void* d_ws, size_t ws_size,
                              hipStream_t stream) {
  fused_decode<<<256, 320, 0, stream>>>(d_in[0], d_in[1], d_in[2], d_in[3], d_ws, d_in[4], d_in[6], d_in[7], d_in[8], d_out);
}
```
